```python
import torch

def _softmax(x, axis=-1):
    return torch.softmax(x, dim=axis)
def _gelu(x):
    return torch.nn.functional.gelu(x, approximate="tanh")
def _silu(x):
    return torch.nn.functional.silu(x)
def _sigmoid(x):
    return torch.sigmoid(x)
def _layernorm(x, axis=-1, eps=1e-5):
    m = x.mean(dim=axis, keepdim=True)
    v = x.var(dim=axis, keepdim=True, unbiased=False)
    return (x - m) / torch.sqrt(v + eps)
def _scatter_add(tgt, idx, src, axis=0):
    return tgt.clone().index_add_(axis, idx, src)
def _segment_sum(src, seg, n):
    out = torch.zeros((n,) + src.shape[1:], dtype=src.dtype)
    return out.index_add_(0, seg, src)
def _pad(x, pw):
    flat = tuple(v for pair in reversed(pw) for v in pair)
    return torch.nn.functional.pad(x, flat)

kernel_name = 'gated_mix__ssm_conv_s4__dense_activation_silu__prompt'

def setup_inputs():

    _w = (lambda a: (lambda t: t.to(torch.bfloat16) if t.is_floating_point() else t)(torch.as_tensor(a)))
    def _A_setup():
        B, L, D, N = 1, 2048, 512, 128
        A_re = -(lambda _x: torch.exp(torch.as_tensor(_x)))((lambda *s: torch.randn(*s, dtype=torch.bfloat16) * 6.4)(D, N))
        A_im = torch.pi * (lambda *s: torch.randn(*s, dtype=torch.bfloat16) * 0.05)(D, N)
        B_re = (lambda *s: torch.randn(*s, dtype=torch.bfloat16) * 0.2)(D, N)
        B_im = (lambda *s: torch.randn(*s, dtype=torch.bfloat16) * 0.2)(D, N)
        C_re = (lambda *s: torch.randn(*s, dtype=torch.bfloat16) * 0.2)(D, N)
        C_im = (lambda *s: torch.randn(*s, dtype=torch.bfloat16) * 0.2)(D, N)
        log_dt = (lambda *s: torch.randn(*s, dtype=torch.bfloat16) * 0.05)(D) * 0.5 - 4.0
        Dskip = (lambda *s: torch.randn(*s, dtype=torch.bfloat16) * 0.05)(D)
        return {
            "u": _w((lambda *s: torch.randn(*s, dtype=torch.bfloat16) * 0.05)(B, L, D)),
            "A_re": _w(A_re),
            "A_im": _w(A_im),
            "B_re": _w(B_re),
            "B_im": _w(B_im),
            "C_re": _w(C_re),
            "C_im": _w(C_im),
            "log_dt": _w(log_dt),
            "Dskip": _w(Dskip),
        }
    _A_in = _A_setup()

    def _B_setup():
        B, S, D, F = 1, 2048, 512, 1792
        return {
            "x": _w((lambda *s: torch.randn(*s, dtype=torch.bfloat16) * 0.05)(B, S, D)),
            "w1": _w((lambda *s: torch.randn(*s, dtype=torch.bfloat16) * 6.4)(D, F) * (D ** -0.5)),
            "w2": _w((lambda *s: torch.randn(*s, dtype=torch.bfloat16) * 0.2)(F, D) * (F ** -0.5)),
        }
    _B_in = _B_setup()

    return {
        "A_u": _A_in["u"],
        "A_A_re": _A_in["A_re"],
        "A_A_im": _A_in["A_im"],
        "A_B_re": _A_in["B_re"],
        "A_B_im": _A_in["B_im"],
        "A_C_re": _A_in["C_re"],
        "A_C_im": _A_in["C_im"],
        "A_log_dt": _A_in["log_dt"],
        "A_Dskip": _A_in["Dskip"],
        "B_x": _B_in["x"],
        "B_w1": _B_in["w1"],
        "B_w2": _B_in["w2"],
        "gate_w": _w((lambda *s: torch.randn(*s, dtype=torch.bfloat16) * 0.05)(512) * 16.0),
    }

def reference(A_u, A_A_re, A_A_im, A_B_re, A_B_im, A_C_re, A_C_im, A_log_dt, A_Dskip, B_x, B_w1, B_w2, gate_w):
    def _A_ref(u, A_re, A_im, B_re, B_im, C_re, C_im, log_dt, Dskip):
        Bsz, L, D = u.shape
        dt = torch.exp(log_dt)[:, None]
        A = A_re + 1j * A_im
        Bc = B_re + 1j * B_im
        Cc = C_re + 1j * C_im
        logdA = dt * A
        dB = dt * Bc
        l = (lambda x, dt: x.to(dt))(torch.arange(L), A_re.dtype)
        P = torch.exp(logdA[:, :, None] * l[None, None, :])
        K = torch.einsum("dn,dnl,dn->dl", Cc, P, dB).real
        L2 = 2 * L
        Kp = _pad(K, ((0, 0), (0, L)))
        up = (lambda x, *d: x.permute(*d))(u, 0, 2, 1)
        up = _pad(up, ((0, 0), (0, 0), (0, L)))
        Kf = (lambda x, axes=(-2,-1): torch.fft.rfft2(x.float(), dim=axes))(Kp[:, None, :], axes=(-2, -1))
        Uf = (lambda x, axes=(-2,-1): torch.fft.rfft2(x.float(), dim=axes))(up[:, :, None, :], axes=(-2, -1))
        Yf = Uf * Kf[None, :, :, :]
        y = (lambda x, s, axes=(-2,-1): torch.fft.irfft2(x, s=s, dim=axes).to(torch.bfloat16))(Yf, (1, L2), axes=(-2, -1))[:, :, 0, :L]
        y = (lambda x, *d: x.permute(*d))(y, 0, 2, 1) + u * Dskip[None, None, :]
        return y

    def _B_ref(x, w1, w2):
        h = torch.einsum("bsd,df->bsf", x, w1)
        h = _silu(h)
        out = torch.einsum("bsf,fd->bsd", h, w2)
        return out

    x = A_u
    g = _sigmoid((x * gate_w).sum(-1, keepdims=True))
    a = _A_ref(x, A_A_re, A_A_im, A_B_re, A_B_im, A_C_re, A_C_im, A_log_dt, A_Dskip)
    b = _B_ref(x, B_w1, B_w2)
    return g * a + (1 - g) * b
```

```cpp
#include <hip/hip_runtime.h>
#include <hip/hip_bf16.h>
#include <stdint.h>

constexpr int L = 2048, D = 512, N = 128, F = 1792;
typedef unsigned short u16;

__device__ __forceinline__ float bf2f(u16 x) { return __uint_as_float(((unsigned)x) << 16); }
__device__ __forceinline__ u16 f2bf(float x) { __hip_bfloat16 b = __float2bfloat16(x); return *reinterpret_cast<u16*>(&b); }
__device__ __forceinline__ float rbf(float x) { return bf2f(f2bf(x)); }

__global__ void kgen(const u16* Are, const u16* Aim, const u16* Bre, const u16* Bim, const u16* Cre, const u16* Cim,
                     const u16* logdt, float* K) {
  int d = blockIdx.x;
  __shared__ float zr[N], zi[N], cr[N], ci[N];
  float dt = rbf(expf(bf2f(logdt[d])));
  if (threadIdx.x < N) {
    int n = threadIdx.x;
    zr[n] = dt * bf2f(Are[d * N + n]);
    zi[n] = dt * bf2f(Aim[d * N + n]);
    float br = dt * bf2f(Bre[d * N + n]), bi = dt * bf2f(Bim[d * N + n]);
    float c0 = bf2f(Cre[d * N + n]), c1 = bf2f(Cim[d * N + n]);
    cr[n] = c0 * br - c1 * bi; ci[n] = c0 * bi + c1 * br;
  }
  __syncthreads();
  for (int l = threadIdx.x; l < L; l += blockDim.x) {
    float lf = rbf((float)l);
    float acc = 0.f;
    for (int n = 0; n < N; ++n) {
      float e = expf(zr[n] * lf);
      float s, c; sincosf(zi[n] * lf, &s, &c);
      acc += e * (cr[n] * c - ci[n] * s);
    }
    K[d * L + l] = acc;
  }
}

__global__ void conv(const u16* u, const float* K, float* y) {
  int d = blockIdx.x;
  __shared__ float ks[L], us[L];
  for (int i = threadIdx.x; i < L; i += blockDim.x) { ks[i] = K[d * L + i]; us[i] = bf2f(u[i * D + d]); }
  __syncthreads();
  for (int l = threadIdx.x; l < L; l += blockDim.x) {
    float acc = 0.f;
    for (int j = 0; j <= l; ++j) acc += ks[j] * us[l - j];
    y[l * D + d] = acc;
  }
}

__global__ void gemm1(const u16* x, const u16* w1, u16* h) {
  int f = blockIdx.x * blockDim.x + threadIdx.x; int l = blockIdx.y;
  __shared__ float xs[D];
  for (int i = threadIdx.x; i < D; i += blockDim.x) xs[i] = bf2f(x[l * D + i]);
  __syncthreads();
  float acc = 0.f;
  for (int k = 0; k < D; ++k) acc += xs[k] * bf2f(w1[k * F + f]);
  float v = rbf(acc);
  float s = v / (1.f + expf(-v));
  h[l * F + f] = f2bf(s);
}

__global__ void gemm2(const u16* u, const u16* h, const u16* w2, const float* y, const u16* Dskip, const u16* gw, u16* out) {
  int d = blockIdx.x * blockDim.x + threadIdx.x; int l = blockIdx.y;
  __shared__ float hs[F];
  __shared__ float red[256];
  for (int i = threadIdx.x; i < F; i += blockDim.x) hs[i] = bf2f(h[l * F + i]);
  float p = 0.f;
  for (int i = threadIdx.x; i < D; i += blockDim.x) p += rbf(bf2f(u[l * D + i]) * bf2f(gw[i]));
  red[threadIdx.x] = p;
  __syncthreads();
  for (int s = 128; s > 0; s >>= 1) { if (threadIdx.x < s) red[threadIdx.x] += red[threadIdx.x + s]; __syncthreads(); }
  float gs = rbf(red[0]);
  float g = rbf(1.f / (1.f + expf(-gs)));
  float acc = 0.f;
  for (int k = 0; k < F; ++k) acc += hs[k] * bf2f(w2[k * D + d]);
  float b = rbf(acc);
  float a = rbf(rbf(y[l * D + d]) + rbf(bf2f(u[l * D + d]) * bf2f(Dskip[d])));
  float o = rbf(g * a) + rbf(rbf(1.f - g) * b);
  out[l * D + d] = f2bf(o);
}

extern "C" void kernel_launch(void* const* d_in, const int* in_sizes, int n_in, void* d_out, int out_size, void* d_ws,
                              size_t ws_size, hipStream_t stream) {
  const u16* u = (const u16*)d_in[0];
  float* K = (float*)d_ws;
  float* y = K + D * L;
  u16* h = (u16*)(y + D * L);
  kgen<<<D, 256, 0, stream>>>((const u16*)d_in[1], (const u16*)d_in[2], (const u16*)d_in[3], (const u16*)d_in[4],
                              (const u16*)d_in[5], (const u16*)d_in[6], (const u16*)d_in[7], K);
  conv<<<D, 256, 0, stream>>>(u, K, y);
  gemm1<<<dim3(F / 256, L), 256, 0, stream>>>(u, (const u16*)d_in[10], h);
  gemm2<<<dim3(D / 256, L), 256, 0, stream>>>(u, h, (const u16*)d_in[11], y, (const u16*)d_in[8], (const u16*)d_in[12], (u16*)d_out);
}
```

```cpp
#define CVPRIO 2
#define PREPRIO 3
#define EPIPRIO 3
#define NCODE 258
#define AGO 5
#define PTC 0
#define KGPRIO 2
#include <hip/hip_runtime.h>
#include <stdint.h>

typedef unsigned short u16;
typedef short s16x4 __attribute__((ext_vector_type(4)));
typedef short s16x8 __attribute__((ext_vector_type(8)));
typedef __bf16 bf16x8 __attribute__((ext_vector_type(8)));
typedef __bf16 bf16x2 __attribute__((ext_vector_type(2)));
typedef float f32x4 __attribute__((ext_vector_type(4)));
typedef float f32x2 __attribute__((ext_vector_type(2)));
typedef unsigned u32x4 __attribute__((ext_vector_type(4)));
typedef unsigned u32x2 __attribute__((ext_vector_type(2)));
typedef __attribute__((address_space(3))) char lds_char;
typedef __attribute__((address_space(3))) s16x4 lds_s16x4;
typedef __attribute__((address_space(3))) s16x8 lds_s16x8;
typedef __attribute__((address_space(3))) u32x4 lds_u32x4;
typedef __attribute__((address_space(3))) unsigned lds_u32;
typedef __attribute__((address_space(3))) u16 lds_u16;
typedef __attribute__((address_space(3))) f32x4 lds_f32x4;

constexpr int L = 2048, D = 512, NM = 128, F = 1792;

#define SCHED0 __builtin_amdgcn_sched_barrier(0)
#define SGB(mask, n) __builtin_amdgcn_sched_group_barrier(mask, n, 0)
__device__ __forceinline__ unsigned long long rt() { return __builtin_amdgcn_s_memrealtime(); }

__device__ __forceinline__ void dma1(unsigned lds, const void* s0, unsigned v0) {
  asm volatile("s_mov_b32 m0, %0\n\ts_nop 0\n\t"
               "global_load_lds_dwordx4 %1, %2 offset:0"
               :: "s"(lds), "v"(v0), "s"(s0) : "memory");
}
__device__ __forceinline__ void dma1c(unsigned lds, const void* s0, unsigned v0) {
  asm volatile("s_mov_b32 m0, %0\n\ts_nop 0\n\t"
               "global_load_lds_dwordx4 %1, %2 offset:0 sc1"
               :: "s"(lds), "v"(v0), "s"(s0) : "memory");
}
__device__ __forceinline__ void dma4(unsigned lds, const void* s0, unsigned v0, unsigned v1, const void* s1, unsigned v2, unsigned v3) {
  asm volatile("s_mov_b32 m0, %0\n\ts_nop 0\n\t"
               "global_load_lds_dwordx4 %1, %5 offset:0\n\t"
               "global_load_lds_dwordx4 %2, %5 offset:1024\n\t"
               "global_load_lds_dwordx4 %3, %6 offset:2048\n\t"
               "global_load_lds_dwordx4 %4, %6 offset:3072"
               :: "s"(lds), "v"(v0), "v"(v1), "v"(v2), "v"(v3), "s"(s0), "s"(s1) : "memory");
}
template <int OFF> __device__ __forceinline__ void dma2(unsigned lds, const void* s0, unsigned v0, unsigned v1) {
  asm volatile("s_mov_b32 m0, %0\n\ts_nop 0\n\t"
               "global_load_lds_dwordx4 %1, %3 offset:%4\n\t"
               "global_load_lds_dwordx4 %2, %3 offset:%5"
               :: "s"(lds), "v"(v0), "v"(v1), "s"(s0), "n"(OFF), "n"(OFF + 1024) : "memory");
}
__device__ __forceinline__ void dma4c(unsigned lds, const void* s0, unsigned v0, unsigned v1, unsigned v2, unsigned v3) {
  asm volatile("s_mov_b32 m0, %0\n\ts_nop 0\n\t"
               "global_load_lds_dwordx4 %1, %5 offset:0 sc1\n\t"
               "global_load_lds_dwordx4 %2, %5 offset:1024 sc1\n\t"
               "global_load_lds_dwordx4 %3, %5 offset:2048 sc1\n\t"
               "global_load_lds_dwordx4 %4, %5 offset:3072 sc1"
               :: "s"(lds), "v"(v0), "v"(v1), "v"(v2), "v"(v3), "s"(s0) : "memory");
}
template <int N> __device__ __forceinline__ void wait_vm() { asm volatile("s_waitcnt vmcnt(%0)" :: "n"(N) : "memory"); }
__device__ __forceinline__ void wait_lgkm() { asm volatile("s_waitcnt lgkmcnt(0)" ::: "memory"); }
__device__ __forceinline__ void bar() { asm volatile("s_barrier" ::: "memory"); }
__device__ __forceinline__ void bar_lgkm() { asm volatile("s_waitcnt lgkmcnt(0)\n\ts_barrier" ::: "memory"); }

__device__ __forceinline__ bf16x8 cat8(s16x4 a, s16x4 b) {
  s16x8 v = __builtin_shufflevector(a, b, 0, 1, 2, 3, 4, 5, 6, 7);
  return __builtin_bit_cast(bf16x8, v);
}
__device__ __forceinline__ s16x4 tr_read(lds_char* p) { return __builtin_amdgcn_ds_read_tr16_b64_v4i16((lds_s16x4*)p); }
__device__ __forceinline__ bf16x8 rd128(lds_char* p) { return __builtin_bit_cast(bf16x8, *(lds_s16x8*)p); }
__device__ __forceinline__ unsigned cvt_pk(float lo, float hi) {
  bf16x2 r = __builtin_convertvector((f32x2{lo, hi}), bf16x2);
  return __builtin_bit_cast(unsigned, r);
}
__device__ __forceinline__ float bflo(unsigned p) { return __uint_as_float(p << 16); }
__device__ __forceinline__ float bfhi(unsigned p) { return __uint_as_float(p & 0xffff0000u); }
__device__ __forceinline__ float rbf(float x) { return bflo(cvt_pk(x, x)); }
__device__ __forceinline__ unsigned silu2(float a, float b) {
  f32x2 v = {a, b};
  f32x2 t = v * -1.44269504088896f;
  f32x2 e = {__builtin_amdgcn_exp2f(t[0]), __builtin_amdgcn_exp2f(t[1])};
  f32x2 d = e + 1.0f;
  f32x2 rr = {__builtin_amdgcn_rcpf(d[0]), __builtin_amdgcn_rcpf(d[1])};
  f32x2 s = v * rr;
  return cvt_pk(s[0], s[1]);
}
__device__ __forceinline__ f32x4 mfma16(bf16x8 a, bf16x8 b, f32x4 c) { return __builtin_amdgcn_mfma_f32_16x16x32_bf16(a, b, c, 0, 0, 0); }
__device__ __forceinline__ void store16_sc1(void* p, u32x4 v) {
  asm volatile("global_store_dwordx4 %0, %1, off sc1\n\ts_nop 1" :: "v"(p), "v"(v) : "memory");
}
__device__ __forceinline__ void store8_sc1(void* p, u32x2 v) {
  asm volatile("global_store_dwordx2 %0, %1, off sc1\n\ts_nop 1" :: "v"(p), "v"(v) : "memory");
}
__device__ __forceinline__ void store4_sc1(void* p, unsigned v) {
  asm volatile("global_store_dword %0, %1, off sc1\n\ts_nop 1" :: "v"(p), "v"(v) : "memory");
}
__device__ __forceinline__ unsigned load4_sc1(const void* p) {
  unsigned v;
  asm volatile("global_load_dword %0, %1, off sc1\n\ts_waitcnt vmcnt(0)" : "=v"(v) : "v"(p) : "memory");
  return v;
}
__device__ __forceinline__ u32x2 load8_sc1(const void* p) {
  u32x2 v;
  asm volatile("global_load_dwordx2 %0, %1, off sc1\n\ts_waitcnt vmcnt(0)" : "=v"(v) : "v"(p) : "memory");
  return v;
}

constexpr size_t WS_HFLAG = 2048, WS_GATE = 16384;
constexpr int U_PITCH = 8448, A_PITCH = 8448;
constexpr size_t WS_UREV = 0x100000, WS_A = 0x600000, WS_H = 0xB00000;

constexpr int SLOT = 32768;
constexpr int WSLOT = 16384;
constexpr int LDS_TAB = 98304, TAB_CH = 29184;
constexpr int TP = 544;
constexpr int K1_OFF = 4640;
constexpr int UB_OFF = 9488, U_CP = 4160;
constexpr int LDS_KC = 156672, KC_CH = 1312;
constexpr int LDS_SYNC = 159296;
constexpr int LDS_UT = 65536;
__host__ __device__ constexpr int lds_at(int c) { return c == 0 ? LDS_TAB + 26624 : c == 1 ? LDS_TAB + TAB_CH + 26624 : c == 2 ? 159744 : 161792; }
constexpr int LDS_TOTAL = 163840;
constexpr int SPIN_MAX = 1 << 20;
constexpr int CODE_LINES = NCODE;
constexpr int AUX_SC1 = 16, RSRC_W3 = 0x00020000;

#define PTS 0
#define PTD 0
#define G2PRIO 0
#define ASLEEP 4
#define S0OFF 0
#define CBROT 0
#define KSTEP(t) (t)
struct TrueT { static constexpr bool value = true; };
struct FalseT { static constexpr bool value = false; };
template <int N> struct IC { static constexpr int value = N; };
template <int A, int B, class Fn> __device__ __forceinline__ void static_for(Fn&& f) {
  if constexpr (A < B) { f(IC<A>{}); static_for<A + 1, B>(f); }
}

__device__ __forceinline__ void cm_n(f32x2 (&o)[4], const f32x2 (&a)[4], const f32x2 (&m)[4]) {
  asm volatile("v_pk_mul_f32 %0, %4, %5 op_sel:[1,1] op_sel_hi:[1,0] neg_lo:[0,1]\n\t"
               "v_pk_mul_f32 %1, %6, %7 op_sel:[1,1] op_sel_hi:[1,0] neg_lo:[0,1]\n\t"
               "v_pk_mul_f32 %2, %8, %9 op_sel:[1,1] op_sel_hi:[1,0] neg_lo:[0,1]\n\t"
               "v_pk_mul_f32 %3, %10, %11 op_sel:[1,1] op_sel_hi:[1,0] neg_lo:[0,1]\n\t"
               "v_pk_fma_f32 %0, %4, %5, %0 op_sel_hi:[0,1,1]\n\t"
               "v_pk_fma_f32 %1, %6, %7, %1 op_sel_hi:[0,1,1]\n\t"
               "v_pk_fma_f32 %2, %8, %9, %2 op_sel_hi:[0,1,1]\n\t"
               "v_pk_fma_f32 %3, %10, %11, %3 op_sel_hi:[0,1,1]"
               : "=&v"(o[0]), "=&v"(o[1]), "=&v"(o[2]), "=&v"(o[3])
               : "v"(a[0]), "v"(m[0]), "v"(a[1]), "v"(m[1]), "v"(a[2]), "v"(m[2]), "v"(a[3]), "v"(m[3]));
}
__device__ __forceinline__ void cm_n(f32x2 (&o)[6], const f32x2 (&a)[6], const f32x2 (&m)[6]) {
  asm volatile("v_pk_mul_f32 %0, %6, %7 op_sel:[1,1] op_sel_hi:[1,0] neg_lo:[0,1]\n\t"
               "v_pk_mul_f32 %1, %8, %9 op_sel:[1,1] op_sel_hi:[1,0] neg_lo:[0,1]\n\t"
               "v_pk_mul_f32 %2, %10, %11 op_sel:[1,1] op_sel_hi:[1,0] neg_lo:[0,1]\n\t"
               "v_pk_mul_f32 %3, %12, %13 op_sel:[1,1] op_sel_hi:[1,0] neg_lo:[0,1]\n\t"
               "v_pk_mul_f32 %4, %14, %15 op_sel:[1,1] op_sel_hi:[1,0] neg_lo:[0,1]\n\t"
               "v_pk_mul_f32 %5, %16, %17 op_sel:[1,1] op_sel_hi:[1,0] neg_lo:[0,1]\n\t"
               "v_pk_fma_f32 %0, %6, %7, %0 op_sel_hi:[0,1,1]\n\t"
               "v_pk_fma_f32 %1, %8, %9, %1 op_sel_hi:[0,1,1]\n\t"
               "v_pk_fma_f32 %2, %10, %11, %2 op_sel_hi:[0,1,1]\n\t"
               "v_pk_fma_f32 %3, %12, %13, %3 op_sel_hi:[0,1,1]\n\t"
               "v_pk_fma_f32 %4, %14, %15, %4 op_sel_hi:[0,1,1]\n\t"
               "v_pk_fma_f32 %5, %16, %17, %5 op_sel_hi:[0,1,1]"
               : "=&v"(o[0]), "=&v"(o[1]), "=&v"(o[2]), "=&v"(o[3]), "=&v"(o[4]), "=&v"(o[5])
               : "v"(a[0]), "v"(m[0]), "v"(a[1]), "v"(m[1]), "v"(a[2]), "v"(m[2]), "v"(a[3]), "v"(m[3]), "v"(a[4]), "v"(m[4]), "v"(a[5]), "v"(m[5]));
}
__device__ __forceinline__ void cm_n(f32x2 (&o)[8], const f32x2 (&a)[8], const f32x2 (&m)[8]) {
  asm volatile("v_pk_mul_f32 %0, %8, %9 op_sel:[1,1] op_sel_hi:[1,0] neg_lo:[0,1]\n\t"
               "v_pk_mul_f32 %1, %10, %11 op_sel:[1,1] op_sel_hi:[1,0] neg_lo:[0,1]\n\t"
               "v_pk_mul_f32 %2, %12, %13 op_sel:[1,1] op_sel_hi:[1,0] neg_lo:[0,1]\n\t"
               "v_pk_mul_f32 %3, %14, %15 op_sel:[1,1] op_sel_hi:[1,0] neg_lo:[0,1]\n\t"
               "v_pk_mul_f32 %4, %16, %17 op_sel:[1,1] op_sel_hi:[1,0] neg_lo:[0,1]\n\t"
               "v_pk_mul_f32 %5, %18, %19 op_sel:[1,1] op_sel_hi:[1,0] neg_lo:[0,1]\n\t"
               "v_pk_mul_f32 %6, %20, %21 op_sel:[1,1] op_sel_hi:[1,0] neg_lo:[0,1]\n\t"
               "v_pk_mul_f32 %7, %22, %23 op_sel:[1,1] op_sel_hi:[1,0] neg_lo:[0,1]\n\t"
               "v_pk_fma_f32 %0, %8, %9, %0 op_sel_hi:[0,1,1]\n\t"
               "v_pk_fma_f32 %1, %10, %11, %1 op_sel_hi:[0,1,1]\n\t"
               "v_pk_fma_f32 %2, %12, %13, %2 op_sel_hi:[0,1,1]\n\t"
               "v_pk_fma_f32 %3, %14, %15, %3 op_sel_hi:[0,1,1]\n\t"
               "v_pk_fma_f32 %4, %16, %17, %4 op_sel_hi:[0,1,1]\n\t"
               "v_pk_fma_f32 %5, %18, %19, %5 op_sel_hi:[0,1,1]\n\t"
               "v_pk_fma_f32 %6, %20, %21, %6 op_sel_hi:[0,1,1]\n\t"
               "v_pk_fma_f32 %7, %22, %23, %7 op_sel_hi:[0,1,1]"
               : "=&v"(o[0]), "=&v"(o[1]), "=&v"(o[2]), "=&v"(o[3]), "=&v"(o[4]), "=&v"(o[5]), "=&v"(o[6]), "=&v"(o[7])
               : "v"(a[0]), "v"(m[0]), "v"(a[1]), "v"(m[1]), "v"(a[2]), "v"(m[2]), "v"(a[3]), "v"(m[3]), "v"(a[4]), "v"(m[4]), "v"(a[5]), "v"(m[5]), "v"(a[6]), "v"(m[6]), "v"(a[7]), "v"(m[7]));
}
__device__ __forceinline__ unsigned cvt_c(f32x2 v) { return cvt_pk(v[0], v[1]); }
template <int R0, int R1> __device__ __forceinline__ void tput2(const unsigned (&tb)[8], unsigned v0, unsigned v1) {
  static_assert((R0 & 7) == (R1 & 7) && 8 * TP == 17 * 256, "row pair");
  asm volatile("ds_write2st64_b32 %0, %1, %2 offset0:%3 offset1:%4" :: "v"(tb[R0 & 7]), "v"(v0), "v"(v1), "n"(17 * (R0 >> 3)), "n"(17 * (R1 >> 3)) : "memory");
}
template <int R> __device__ __forceinline__ void tput1(const unsigned (&tb)[8], unsigned v) {
  asm volatile("ds_write_b32 %0, %1 offset:%2" :: "v"(tb[0]), "v"(v), "n"(R * TP) : "memory");
}
__device__ __forceinline__ f32x2 sexp(float zr, float zi, float k) {
  float m = __builtin_amdgcn_exp2f(zr * (k * 1.44269504088896f));
  float ph = zi * (k * 0.15915494309189535f);
  ph = ph - __builtin_rintf(ph);
  return f32x2{m * __builtin_amdgcn_cosf(ph), m * __builtin_amdgcn_sinf(ph)};
}
__device__ __forceinline__ lds_char* opq(lds_char* p) { asm volatile("" : "+v"(p)); return p; }
__device__ __forceinline__ unsigned lds_ld(lds_char* p) { return *(volatile lds_u32*)p; }
__device__ __forceinline__ void lds_st(lds_char* p, unsigned v) { *(volatile lds_u32*)p = v; }
__device__ __forceinline__ unsigned ld_ushort(const u16* p) {
  unsigned v;
  asm volatile("global_load_ushort %0, %1, off" : "=v"(v) : "v"(p) : "memory");
  return v;
}
__device__ __forceinline__ unsigned ld_dword(const void* p) {
  unsigned v;
  asm volatile("global_load_dword %0, %1, off" : "=v"(v) : "v"(p) : "memory");
  return v;
}
__device__ __forceinline__ unsigned ld_dword_sc1(const void* p) {
  unsigned v;
  asm volatile("global_load_dword %0, %1, off sc1" : "=v"(v) : "v"(p) : "memory");
  return v;
}

__device__ __forceinline__ unsigned landed(unsigned v) {
  unsigned o;
  asm volatile("v_mov_b32 %0, %1" : "=v"(o) : "v"(v));
  return o;
}

__device__ __forceinline__ void csync(lds_char* cnt, int lane) {
  wait_lgkm();
  if (lane == 0) __hip_atomic_fetch_add((lds_u32*)cnt, 1u, __ATOMIC_RELAXED, __HIP_MEMORY_SCOPE_WORKGROUP);
  for (int it = 0; it < SPIN_MAX; ++it)
    if (__builtin_amdgcn_readfirstlane(lds_ld(cnt)) >= 2) break;
  asm volatile("" ::: "memory");
}
__device__ __forceinline__ void poll_until(const char* q, unsigned long long mask, unsigned tag) {
  for (int it = 0; it < SPIN_MAX; ++it) {
    unsigned v = load4_sc1(q);
    if ((__builtin_amdgcn_ballot_w64(v != tag) & mask) == 0) break;
  }
}

__global__ __launch_bounds__(512, 2) void fused_kernel(
    const u16* __restrict__ x, const u16* __restrict__ Are, const u16* __restrict__ Aim, const u16* __restrict__ Bre,
    const u16* __restrict__ Bim, const u16* __restrict__ Cre, const u16* __restrict__ Cim, const u16* __restrict__ logdt,
    const u16* __restrict__ Dsk, const u16* __restrict__ w1, const u16* __restrict__ w2, const u16* __restrict__ gw,
    u16* __restrict__ out, char* __restrict__ ws
    ) {
  __shared__ __attribute__((aligned(16))) char smem[LDS_TOTAL];
  const char* pc0;
  asm volatile("s_getpc_b64 %0" : "=s"(pc0));
  lds_char* sm = (lds_char*)smem;
  const unsigned smb = (unsigned)(uintptr_t)sm;
  const int tid = threadIdx.x, lane = tid & 63;
  const int w = __builtin_amdgcn_readfirstlane(tid >> 6);
  const int b = blockIdx.x, xcd = b & 7, r = b >> 3;
  const int g = lane >> 4, j = lane & 15;
  const bool isG1 = r < 28;
  const int rb = 4 * (xcd >> 1) + r / 7, ft = 7 * (xcd & 1) + r % 7;
  const int rb64 = xcd * 4 + (isG1 ? r / 7 : r - 28);
  const int cb = (3 - (int)((uintptr_t)w2 >> 7) + (isG1 ? 1 + r % 7 : 0)) & 7;
  const int chl = (w >> 1) & 1, hf = w & 1;
  const int ch = 2 * b + chl;
  const int pidx = xcd * 4 + (r - 28);
  const int s0 = ((ft >> 1) + 2 * (r / 7)) & 7;
  unsigned vW[4];
  vW[0] = (lane >> 3) * 1024 + (((lane & 7) ^ (((lane >> 4) & 3) << 1)) << 4);
  #pragma unroll
  for (int i = 1; i < 4; ++i) vW[i] = vW[0] + i * 7168;
  const unsigned wlds = __builtin_amdgcn_readfirstlane(smb + (w & 3) * 4096);
  auto w2dma = [&](int kq, int t, int slot) {
    const char* src = (const char*)w2 + cb * 128 + (size_t)(kq * 14 + KSTEP(t)) * 32768;
    dma4(wlds + slot * WSLOT, src, vW[0], vW[1], src, vW[2], vW[3]);
  };
  auto ptouch = [&](unsigned& dst, int t) {
    const int n = (r - 28) * 256 + (w & 3) * 64 + lane;
    const char* q = (const char*)w2 + (size_t)((n >> 8) * 14 + t) * 32768 + (n & 255) * 128;
    asm volatile("global_load_dword %0, %1, off" : "+v"(dst) : "v"(q) : "memory");
  };
  const int hw = w & 3;
  unsigned vP[8];
  {
    int rowA = (lane >> 4), posA = lane & 15, q4 = (lane >> 4) << 2;
    int rowB = (lane >> 3), posB = lane & 7, cB = posB ^ (lane >> 3);
    vP[0] = rowA * (F * 2) + ((posA ^ (q4 | 0)) << 4) + 3072;
    vP[1] = (rowA + 4) * (F * 2) + ((posA ^ (q4 | 1)) << 4) + 3072 - 1024;
    vP[2] = rowB * (D * 2) + (cB << 4) + 3072 - 2048;
    vP[3] = (rowB + 8) * (D * 2) + (cB << 4);
    vP[4] = (rowA + 8) * (F * 2) + ((posA ^ (q4 | 2)) << 4) + 3072;
    vP[5] = (rowA + 12) * (F * 2) + ((posA ^ (q4 | 3)) << 4) + 3072 - 1024;
    vP[6] = vP[2] + 16 * (D * 2);
    vP[7] = vP[3] + 16 * (D * 2);
  }
  const char* gA = (const char*)w1 + ft * 256 + (size_t)(16 * hw) * (F * 2) - 3072;
  const char* gB = (const char*)x + (size_t)(rb * 128 + 32 * hw) * (D * 2) - 3072;
  auto half_stage = [&](int s, int h) {
    const int sl = (s + s0) & 7;
    dma4(smb + hw * 8192 + (s % 3) * SLOT + h * 4096, gA + (size_t)sl * 64 * (F * 2), vP[4 * h], vP[4 * h + 1],
         gB + sl * 128, vP[4 * h + 2], vP[4 * h + 3]);
  };
  auto stage = [&](int s) { half_stage(s, 0); half_stage(s, 1); };
  auto piece = [&](int s, int i) {
    const int sl = (s + s0) & 7;
    const char* src = (i & 2) == 0 ? gA + (size_t)sl * 64 * (F * 2) : gB + sl * 128;
    asm volatile("s_mov_b32 m0, %0\n\ts_nop 0\n\tglobal_load_lds_dwordx4 %1, %2 offset:%3"
                 :: "s"(smb + hw * 8192 + (s % 3) * SLOT + (i >> 2) * 4096), "v"(vP[i]), "s"(src), "n"((i & 3) * 1024) : "memory");
  };
  unsigned tag, touch0 = 0, touchx = 0;
  float ds = 0.f;
  const unsigned ta0 = smb + LDS_TAB + chl * TAB_CH + (64 * hf + lane) * 4;
  lds_char* kb = sm + LDS_TAB + chl * TAB_CH;
  unsigned tagv, pds = 0;
  u32x2 ut[4];
  float gpart;
  unsigned uvo, gvo;
  const char* ubase = ws + WS_UREV + (size_t)(64 * cb + 32 * ((w >> 1) & 1)) * U_PITCH + 4 * (2016 - 64 * rb64 - 32 * (w & 1));
  const char* gbase = ws + WS_GATE + (size_t)(cb * 2048 + 64 * rb64 + 32 * (w & 1) + 16 * ((w >> 1) & 1)) * 8;
  auto ustore = [&](int i) {
    const u32x2 d = ut[i];
    const unsigned d0 = d[0], d1 = d[1];
    asm volatile("global_store_dwordx4 %0, %1, %2 sc1\n\ts_nop 1" :: "v"(uvo), "v"(u32x4{d0, tag, d1, tag}), "s"(ubase + (size_t)(8 * i) * U_PITCH) : "memory");
  };
  auto gstore = [&]() {
    if (j < 4) asm volatile("global_store_dwordx2 %0, %1, %2 sc1\n\ts_nop 1" :: "v"(gvo), "v"(u32x2{__float_as_uint(gpart), tag}), "s"(gbase) : "memory");
  };
  asm volatile("" :: "s"(Are), "s"(Aim), "s"(Bre), "s"(Bim), "s"(Cre), "s"(Cim), "s"(logdt), "s"(Dsk), "s"(x), "s"(w1), "s"(w2), "s"(gw), "s"(ws));
  if (w >= 4) {
    if (isG1) { stage(0); stage(1); }
    tagv = ld_dword_sc1(ws);
    pds = ld_ushort(Dsk + ch);
    if (lane < 16 && w == 4) lds_st(sm + LDS_SYNC + lane * 4, 0);
  } else {
    tagv = ld_dword_sc1(ws);
    const size_t po = (size_t)ch * NM + 64 * hf + lane;
    const unsigned pa_r = ld_ushort(Are + po), pa_i = ld_ushort(Aim + po), pdt = ld_ushort(logdt + ch);
    const unsigned pb_r = ld_ushort(Bre + po), pb_i = ld_ushort(Bim + po), pc_r = ld_ushort(Cre + po), pc_i = ld_ushort(Cim + po);
    u32x4 gwv[2];
    #pragma unroll
    for (int ks = 0; ks < 2; ++ks)
      asm volatile("global_load_dwordx4 %0, %1, off offset:%2" : "=v"(gwv[ks]) : "v"((const char*)gw + cb * 128 + g * 16), "n"(ks * 64) : "memory");
    #pragma unroll
    for (int i = 0; i < 4; ++i)
      dma1(smb + LDS_UT + w * 4096 + i * 1024, (const char*)x + (size_t)(64 * rb64 + 32 * (w & 1) + 8 * i) * 1024 + cb * 128,
           (lane >> 3) * 1024 + (((lane & 7) ^ (lane >> 3)) << 4));
    unsigned tb[8];
    #pragma unroll
    for (int i = 0; i < 8; ++i) { tb[i] = ta0 + i * TP; asm volatile("" : "+v"(tb[i])); }
    unsigned utr[4], urd[2];
    {
      const int row = 16 * ((lane >> 1) & 1) + 4 * g + 3 - ((lane >> 2) & 3), grow = 16 * (w >> 1) + j;
      #pragma unroll
      for (int i = 0; i < 4; ++i) utr[i] = smb + LDS_UT + w * 4096 + row * 128 + (((4 * (w >> 1) + i) ^ (row & 7)) << 4) + 8 * (lane & 1);
      #pragma unroll
      for (int i = 0; i < 2; ++i) urd[i] = smb + LDS_UT + w * 4096 + grow * 128 + (((4 * i + g) ^ (grow & 7)) << 4);
    }
    uvo = (j & 7) * U_PITCH + 4 * (28 - 16 * (j >> 3) - 4 * g); gvo = (4 * g + j) * 8;
    asm volatile("" : "+v"(utr[0]), "+v"(utr[1]), "+v"(utr[2]), "+v"(utr[3]), "+v"(urd[0]), "+v"(urd[1]), "+v"(uvo), "+v"(gvo));
#define XN 0
    if (isG1) {
      int n = (r % 7) * 55 + w * 14 + (lane < 13 ? lane : 13);
      if (n > 383) n = 383;
      int sl = (2 * (r / 7) + 5 + 3 * (xcd & 1) + (n >> 7)) & 7;
      touch0 = ld_dword((const char*)x + (size_t)(rb * 128 + (n & 127)) * 1024 + sl * 128);
      wait_vm<5 + XN>();
    } else {
      wait_vm<4 + XN>();
    }
    tag = __builtin_amdgcn_readfirstlane(landed(tagv)) + 1;
    const float dt = rbf(__builtin_amdgcn_exp2f(bflo(landed(pdt)) * 1.44269504088896f));
    const float zr = dt * bflo(landed(pa_r)), zi = dt * bflo(landed(pa_i));
    const f32x2 dB{dt * bflo(landed(pb_r)), dt * bflo(landed(pb_i))}, cC{bflo(landed(pc_r)), bflo(landed(pc_i))};
    auto tables = [&](float zr, float zi, auto mid) {
      const f32x2 e8 = sexp(zr, zi, 8.f), e1 = sexp(zr, -zi, 1.f), e256 = sexp(zr, -zi, 256.f);
      f32x2 a4[4], a8[8], a6[6];
      cm_n(a4, {e8, e1, e256, cC}, {e8, e1, e256, dB});
      const f32x2 e16 = a4[0], m2 = a4[1], m512 = a4[2], l0 = a4[3];
      cm_n(a8, {e16, l0, l0, m2, m2, m512, m512, e256}, {e16, e8, e16, e1, m2, e256, m512, m2});
      const f32x2 e32 = a8[0], l1 = a8[1], l2 = a8[2], e3 = a8[3], m4 = a8[4], e768 = a8[5], e1024 = a8[6], w41 = a8[7];
      tput2<32, 40>(tb, 0x3f80u, cvt_c(e256));
      tput2<33, 41>(tb, cvt_c(e1), cvt_c(w41));
      tput2<36, 44>(tb, cvt_c(m4), cvt_c(m512));
      f32x2 b8[8];
      cm_n(b8, {e32, l1, e3, e1, e3, e256, m512, e768}, {e32, e16, e3, m4, m4, m4, m4, m4});
      const f32x2 e64 = b8[0], l3 = b8[1], e6 = b8[2];
      tput2<34, 42>(tb, cvt_c(m2), cvt_c(b8[5]));
      tput2<37, 45>(tb, cvt_c(b8[3]), cvt_c(b8[6]));
      tput2<38, 46>(tb, cvt_c(e6), cvt_c(e768));
      tput2<39, 47>(tb, cvt_c(b8[4]), cvt_c(b8[7]));
      cm_n(a6, {e64, e6, e256, m512, e768, e1024}, {e64, e256, e1024, e1024, e1024, e1024});
      const f32x2 e128 = a6[0];
      tput2<35, 43>(tb, cvt_c(e3), cvt_c(a6[1]));
      tput1<48>(tb, cvt_c(e1024)); tput1<49>(tb, cvt_c(a6[2])); tput1<50>(tb, cvt_c(a6[3]));
      tput1<51>(tb, cvt_c(a6[4])); tput1<52>(tb, cvt_c(a6[5]));
      mid();
      f32x2 lh[8];
      cm_n(a4, {l0, l1, l2, l3}, {e128, e128, e128, e128});
      lh[0] = l0; lh[1] = l1; lh[2] = l2; lh[3] = l3; lh[4] = a4[0]; lh[5] = a4[1]; lh[6] = a4[2]; lh[7] = a4[3];
      static_for<0, 4>([&](auto ST) {
        constexpr int st = decltype(ST)::value;
        unsigned c[8];
        #pragma unroll
        for (int i = 0; i < 8; ++i) c[i] = cvt_c(lh[i]);
        f32x2 nx[8];
        if constexpr (st < 3) cm_n(nx, lh, {e32, e32, e32, e32, e32, e32, e32, e32});
        tput2<4 * st, 16 + 4 * st>(tb, c[0], c[4]); tput2<4 * st + 1, 17 + 4 * st>(tb, c[1], c[5]);
        tput2<4 * st + 2, 18 + 4 * st>(tb, c[2], c[6]); tput2<4 * st + 3, 19 + 4 * st>(tb, c[3], c[7]);
        if constexpr (st < 3) {
          #pragma unroll
          for (int i = 0; i < 8; ++i) lh[i] = nx[i];
        }
      });
    };
    bf16x8 ua[2];
    tables(zr, zi, [&]() {
      if (isG1) wait_vm<1 + XN>(); else wait_vm<XN>();
      #pragma unroll
      for (int i = 0; i < 4; ++i)
        asm volatile("ds_read_b64_tr_b16 %0, %1" : "=v"(ut[i]) : "v"(utr[i]) : "memory");
      #pragma unroll
      for (int i = 0; i < 2; ++i)
        asm volatile("ds_read_b128 %0, %1" : "=v"(ua[i]) : "v"(urd[i]) : "memory");
    });
    asm volatile("s_waitcnt lgkmcnt(0)" : "+v"(ut[0]), "+v"(ut[1]), "+v"(ut[2]), "+v"(ut[3]), "+v"(ua[0]), "+v"(ua[1]), "+v"(gwv[0]), "+v"(gwv[1]) :: "memory");
    f32x4 lg = {0, 0, 0, 0};
    #pragma unroll
    for (int ks = 0; ks < 2; ++ks) lg = mfma16(ua[ks], __builtin_bit_cast(bf16x8, gwv[ks]), lg);
    gpart = (j & 2) ? ((j & 1) ? lg[3] : lg[2]) : ((j & 1) ? lg[1] : lg[0]);
    asm volatile("" : "+v"(gpart));
    ustore(0); ustore(1); ustore(2); ustore(3); gstore();
  }

  if (w < 4) {
    const int kq = w;
    unsigned touch2 = 0, touch3 = 0;
    const char* hown = ws;
    if (isG1) {
      const int fh = w >> 1, lh = w & 1;
      unsigned aA[4][2], aB[2];
      {
        int q = (lane >> 2) & 3, p = lane & 3;
        #pragma unroll
        for (int fb = 0; fb < 4; ++fb)
          #pragma unroll
          for (int t = 0; t < 2; ++t) {
            int chunk = 8 * fh + 2 * fb + (p >> 1);
            int mask = (q << 2) | ((2 * g + t) & 3);
            aA[fb][t] = g * 4096 + (4 * t + q) * 256 + ((chunk ^ mask) << 4) + 8 * (p & 1);
          }
        #pragma unroll
        for (int ks = 0; ks < 2; ++ks) aB[ks] = (4 * lh) * 4096 + 2048 + j * 128 + ((((4 * ks + g) ^ (j & 7))) << 4);
      }
      bf16x8 fa[2][4], fb_[2][4];
      auto rd = [&](int k) {
        int s = k >> 1, ks = k & 1, n = k & 1;
        lds_char* base = sm + (s % 3) * SLOT;
        fb_[n][0] = rd128(base + aB[ks]);
        fa[n][0] = cat8(tr_read(base + ks * 16384 + aA[0][0]), tr_read(base + ks * 16384 + aA[0][1]));
        fb_[n][1] = rd128(base + 4096 + aB[ks]);
        fb_[n][2] = rd128(base + 8192 + aB[ks]);
        fb_[n][3] = rd128(base + 12288 + aB[ks]);
        #pragma unroll
        for (int i = 1; i < 4; ++i) fa[n][i] = cat8(tr_read(base + ks * 16384 + aA[i][0]), tr_read(base + ks * 16384 + aA[i][1]));
      };
      f32x4 acc[4][4];
      #pragma unroll
      for (int i = 0; i < 4; ++i)
        #pragma unroll
        for (int jj = 0; jj < 4; ++jj) acc[i][jj] = f32x4{0, 0, 0, 0};
      bar_lgkm();
      rd(0);
      static_for<0, 16>([&](auto KT) {
        constexpr int k = decltype(KT)::value;
        if (k & 1) bar_lgkm();
        if (k == 15) { w2dma(kq, 4, 2); w2dma(kq, 5, 3); }
        SCHED0;
        if (k + 1 < 16) rd(k + 1);
        #pragma unroll
        for (int i = 0; i < 4; ++i)
          #pragma unroll
          for (int lb = 0; lb < 4; ++lb) acc[i][lb] = mfma16(fa[k & 1][i], fb_[k & 1][lb], acc[i][lb]);
        if (k + 1 < 16) {
          #pragma unroll
          for (int i = 0; i < 12; ++i) { SGB(0x008, 1); SGB(0x100, 1); }
          SGB(0x008, 4);
        }
        SCHED0;
      });
      __builtin_amdgcn_s_setprio(EPIPRIO);
      char* hb = ws + WS_H + ((size_t)(8 * rb + 4 * lh) * 56 + 4 * ft + 2 * fh) * 1024 + lane * 16;
      #pragma unroll
      for (int p = 0; p < 2; ++p) {
        #pragma unroll
        for (int lb = 0; lb < 4; ++lb) {
          u32x4 v;
          v[0] = silu2(acc[2 * p][lb][0], acc[2 * p][lb][1]);
          v[1] = silu2(acc[2 * p][lb][2], acc[2 * p][lb][3]);
          v[2] = silu2(acc[2 * p + 1][lb][0], acc[2 * p + 1][lb][1]);
          v[3] = silu2(acc[2 * p + 1][lb][2], acc[2 * p + 1][lb][3]);
          store16_sc1(hb + ((size_t)lb * 56 + p) * 1024, v);
        }
      }
      hown = hb - lane * 16 + ((lane >> 4) * 56 + ((lane >> 3) & 1)) * 1024 + (lane & 7) * 128;
    } else {
      bar_lgkm();
      static_for<PTS, PTS + PTC>([&](auto IT) { ptouch(touch2, decltype(IT)::value); });
      w2dma(kq, 4, 2); w2dma(kq, 5, 3);
    }
    {
      unsigned aW[4];
      {
        const int q = (lane >> 2) & 3, p = lane & 3, swz = ((2 * g + (q >> 1)) & 3) << 1;
        #pragma unroll
        for (int c = 0; c < 4; ++c) aW[c] = (kq * 4096 + (4 * g + q) * 128 + (((p >> 1) ^ swz) << 4) + 8 * (p & 1)) ^ (c << 5);
      }
      bf16x8 A[5][4];
      auto rdA = [&](int t) {
        #pragma unroll
        for (int c = 0; c < 4; ++c)
          A[t % 5][c] = cat8(tr_read(sm + ((t + 4) % 6) * WSLOT + aW[c]), tr_read(sm + ((t + 4) % 6) * WSLOT + aW[c] + 2048));
      };
      auto dmaW = [&](int t) { w2dma(kq, t, (t + 4) % 6); };
      __builtin_amdgcn_s_setprio(PREPRIO);
      wait_vm<0>();
      asm volatile("" :: "v"(touch0), "v"(touch2), "v"(touch3), "v"(touchx));
      if (isG1 && lane == 0) store4_sc1(ws + WS_HFLAG + (size_t)(2 * rb + (w & 1)) * 256 + (2 * ft + (w >> 1)) * 4, tag);
      for (int it = 0; it < SPIN_MAX; ++it)
        if (__builtin_amdgcn_readfirstlane(lds_ld(sm + LDS_SYNC + 16 + kq * 4)) != 0) break;
      asm volatile("" ::: "memory");
      rdA(0); rdA(1); rdA(2); rdA(3);
      unsigned pf0;
      wait_lgkm();
      dmaW(6); dmaW(7);
      const char* fl = ws + WS_HFLAG + (size_t)rb64 * 256 + (7 * kq + (lane < 7 ? lane : 0)) * 4;
      asm volatile("global_load_dword %0, %1, off sc1" : "=v"(pf0) : "v"(fl) : "memory");
      dmaW(8); dmaW(9);
      const char* hq = ws + WS_H + (size_t)rb64 * 229376 + kq * 14336;
      unsigned vH[4];
      #pragma unroll
      for (int lb = 0; lb < 4; ++lb) vH[lb] = lane * 16 + ((kq + 1 + lb) & 3) * 57344;
      asm volatile("" : "+v"(vH[0]), "+v"(vH[1]), "+v"(vH[2]), "+v"(vH[3]));
      asm volatile("s_waitcnt vmcnt(8)" : "+v"(pf0) :: "memory");
      if (__builtin_amdgcn_ballot_w64(pf0 != tag) != 0) {
        for (int it = 0; it < SPIN_MAX; ++it) {
          unsigned v = load4_sc1(fl);
          if (__builtin_amdgcn_ballot_w64(v != tag) == 0) break;
        }
      }
      u32x4 B[4][4];
#define B_LAST B[1]
      auto ldB = [&](int t) {
        const char* src = hq + (KSTEP(t)) * 1024;
        #pragma unroll
        for (int lb = 0; lb < 4; ++lb)
          asm volatile("global_load_dwordx4 %0, %1, %2 sc1" : "=v"(B[t % 4][lb]) : "v"(vH[lb]), "s"(src) : "memory");
      };
      ldB(0); ldB(1); ldB(2);
      unsigned th = 0;
      {
        const int n = cb * 44 + (lane < 43 ? lane : 43);
        asm volatile("global_load_dword %0, %1, off sc1" : "=v"(th) : "v"(hq + (n / 88) * 57344 + 3072 + (n % 88) * 128) : "memory");
      }
      constexpr int TH = 1;
      f32x4 acc[4][4];
      #pragma unroll
      for (int c = 0; c < 4; ++c)
        #pragma unroll
        for (int lb = 0; lb < 4; ++lb) acc[c][lb] = f32x4{0, 0, 0, 0};
      __builtin_amdgcn_s_setprio(G2PRIO);
      constexpr int T_END = 12;
      static_for<0, T_END>([&](auto TT) {
        constexpr int t = decltype(TT)::value;
        constexpr int wn = t == 0 ? 8 + TH : t == 1 ? 8 + TH : t == 2 ? 12 + TH : t <= 5 ? 16 : t == 6 ? 12 : t <= 11 ? 8 : t == 12 ? 4 : 0;
        constexpr int ta = t + 4;
        asm volatile("s_waitcnt vmcnt(%4)" : "+v"(B[t % 4][0]), "+v"(B[t % 4][1]), "+v"(B[t % 4][2]), "+v"(B[t % 4][3]) : "n"(wn));
        if (t == 3) asm volatile("" :: "v"(th));
        if (t >= 1 && t <= 4) { wait_lgkm(); dmaW(t + 9); }
        if (t + 3 < 14) ldB(t + 3);
        if (t == AGO && lane == 0) lds_st(sm + LDS_SYNC + 48 + kq * 4, 1);
        SCHED0;
        if (ta < 14) rdA(ta);
        #pragma unroll
        for (int lb = 0; lb < 4; ++lb)
          #pragma unroll
          for (int c = 0; c < 4; ++c) acc[c][lb] = mfma16(A[t % 5][c], __builtin_bit_cast(bf16x8, B[t % 4][lb]), acc[c][lb]);
        if (ta < 14) {
          #pragma unroll
          for (int i = 0; i < 8; ++i) { SGB(0x008, 1); SGB(0x100, 1); }
          SGB(0x008, 8);
        }
        SCHED0;
      });
      int ln;
      asm volatile("v_mbcnt_lo_u32_b32 %0, -1, 0\n\tv_mbcnt_hi_u32_b32 %0, -1, %0" : "=v"(ln));
      asm volatile("s_waitcnt vmcnt(4)" : "+v"(B[0][0]), "+v"(B[0][1]), "+v"(B[0][2]), "+v"(B[0][3]));
      lds_char* rp = opq(sm + 2 * WSLOT + kq * 4096 + ln * 16);
      const int g2 = ln >> 4, j2 = ln & 15;
      lds_char* gp = opq(sm + LDS_SYNC + 64 + w * 64 + j2 * 4);
      lds_char* ap[4];
      {
        const int q = (ln >> 2) & 3, p = ln & 3, swz = ((2 * g2 + (q >> 1)) & 3) << 1;
        #pragma unroll
        for (int c = 0; c < 4; ++c) ap[c] = opq(sm + (4 * g2 + q) * 128 + (((2 * w + (p >> 1)) ^ swz) << 4) + 8 * (p & 1) + lds_at(c));
      }
      lds_char* pp[3];
      #pragma unroll
      for (int o = 1; o < 4; ++o) pp[o - 1] = opq(sm + (5 - o) * WSLOT + ((w + o) & 3) * 4096 + ln * 16);
      char* op = (char*)out + (size_t)(64 * rb64 + 16 * w + j2) * 1024 + cb * 128 + (g2 & 1) * 32 + (g2 >> 1) * 16;
      asm volatile("" : "+v"(op));
      static_for<0, 4>([&](auto LB) {
        constexpr int lb = decltype(LB)::value;
        #pragma unroll
        for (int c = 0; c < 4; ++c) acc[c][lb] = mfma16(A[12 % 5][c], __builtin_bit_cast(bf16x8, B[0][lb]), acc[c][lb]);
        asm volatile("s_waitcnt vmcnt(%1)" : "+v"(B_LAST[lb]) : "n"(3 - lb));
        #pragma unroll
        for (int c = 0; c < 4; ++c) acc[c][lb] = mfma16(A[13 % 5][c], __builtin_bit_cast(bf16x8, B_LAST[lb]), acc[c][lb]);
        if (lb > 0) {
          #pragma unroll
          for (int c = 0; c < 4; ++c) *(lds_f32x4*)(rp + (lb - 1) * WSLOT + c * 1024) = acc[c][lb - 1];
          SGB(0x008, 4);
          #pragma unroll
          for (int i = 0; i < 4; ++i) { SGB(0x008, 1); SGB(0x200, 1); }
        } else {
          #pragma unroll
          for (int i = 0; i < 8; ++i) { SGB(0x008, 1); SGB(0x006, 7); }
        }
        SCHED0;
      });
      bar_lgkm();
      const float gv = *(__attribute__((address_space(3))) float*)gp;
      s16x4 av[4];
      #pragma unroll
      for (int c = 0; c < 4; ++c) av[c] = tr_read(ap[c]);
      SCHED0;
      f32x4 part[3][4];
      #pragma unroll
      for (int c = 0; c < 4; ++c)
        #pragma unroll
        for (int o = 0; o < 3; ++o) part[o][c] = *(lds_f32x4*)(pp[o] + c * 1024);
      SCHED0;
      const float gm = rbf(1.0f - gv);
      unsigned ov[4][2];
      f32x2 ga[4][2];
      #pragma unroll
      for (int c = 0; c < 4; ++c) {
        u32x2 au = __builtin_bit_cast(u32x2, av[c]);
        #pragma unroll
        for (int hh = 0; hh < 2; ++hh) ga[c][hh] = f32x2{bflo(au[hh]), bfhi(au[hh])} * gv;
      }
      SCHED0;
      #pragma unroll
      for (int c = 0; c < 4; ++c) {
        const f32x4 own = ((acc[c][3] + part[0][c]) + part[1][c]) + part[2][c];
        #pragma unroll
        for (int hh = 0; hh < 2; ++hh) {
          const f32x2 o = f32x2{own[2 * hh], own[2 * hh + 1]} * gm + ga[c][hh];
          ov[c][hh] = cvt_pk(o[0], o[1]);
        }
        SCHED0;
      }
      #pragma unroll
      for (int pr = 0; pr < 2; ++pr) {
        u32x2 s0v = __builtin_amdgcn_permlane16_swap(ov[2 * pr][0], ov[2 * pr + 1][0], false, false);
        u32x2 s1v = __builtin_amdgcn_permlane16_swap(ov[2 * pr][1], ov[2 * pr + 1][1], false, false);
        { u32x4 ovv{s0v[0], s1v[0], s0v[1], s1v[1]}; asm volatile("global_store_dwordx4 %0, %1, off sc0 sc1\n\ts_nop 1" :: "v"(op + pr * 64), "v"(ovv) : "memory"); }
      }
    }
  } else {
    unsigned wtouch = 0;
    constexpr int DX = 2;
    if (isG1) {
      wait_vm<8 + DX>();
      bar();
      static_for<2, 8>([&](auto ST) {
        constexpr int s = decltype(ST)::value;
        stage(s);
        wait_vm<(s == 2 ? 8 + DX : 8)>();
        bar();
      });
    } else {
#define PSLEEP0 28
      { const int n = ((r - 28) * 4 + hw) * 17 + (lane < 16 ? lane : 16);
        asm volatile("global_load_dword %0, %1, off" : "+v"(wtouch) : "v"(pc0 + (n < CODE_LINES ? n : CODE_LINES - 1) * 128) : "memory"); }
      if (PSLEEP0) __builtin_amdgcn_s_sleep(PSLEEP0);
      #pragma unroll
      for (int i = 0; i < 2; ++i) {
        const int n = (pidx * 4 + hw) * 112 + 56 * i + (lane < 55 ? lane : 55);
        asm volatile("global_load_dword %0, %1, off" : "+v"(wtouch) : "v"((const char*)w2 + (size_t)n * 128) : "memory");
      }
      bar_lgkm();
    }
    w2dma(hw, 0, 4); w2dma(hw, 1, 5);
    wait_vm<8>();
    if (isG1) bar_lgkm();
    tag = __builtin_amdgcn_readfirstlane(landed(tagv)) + 1;
    ds = bflo(landed(pds));
    w2dma(hw, 2, 0); w2dma(hw, 3, 1);
    const char* uq = ws + WS_UREV + (size_t)ch * U_PITCH + hf * 4096 + lane * 16;
    u32x4 ur[4];
    auto uload = [&]() {
      #pragma unroll
      for (int i = 0; i < 4; ++i)
        asm volatile("global_load_dwordx4 %0, %1, off offset:%2 sc1" : "=v"(ur[i]) : "v"(uq), "n"(i * 1024) : "memory");
    };
    uload();
    if (isG1) bar();
    constexpr int DT = 0;
    __builtin_amdgcn_s_setprio(KGPRIO);
    {
      lds_u16* kc = (lds_u16*)(sm + LDS_KC + chl * KC_CH);
      lds_char* ra = opq(kb + j * TP + g * 16);
      lds_char* re = opq(ra + hf * (16 * TP));
      f32x4 d0 = {0, 0, 0, 0}, d1 = {0, 0, 0, 0};
      bf16x8 fe[8], fw0[8], fw1[8];
      #pragma unroll
      for (int ks = 0; ks < 8; ++ks) { fe[ks] = rd128(re + ks * 64); fw0[ks] = rd128(ra + 32 * TP + ks * 64); fw1[ks] = rd128(ra + 48 * TP + ks * 64); }
      #pragma unroll
      for (int ks = 0; ks < 8; ++ks) { d0 = mfma16(fw0[ks], fe[ks], d0); d1 = mfma16(fw1[ks], fe[ks], d1); }
      const int rr = 16 * hf + j;
      unsigned p01 = cvt_pk(d0[0], d0[1]), p23 = cvt_pk(d0[2], d0[3]);
      int q0 = g < 2 ? 4 * g + 8 * rr : (g == 2 ? 256 + 4 * rr : 384 + 2 * rr);
      *(lds_u32*)(kc + q0) = p01;
      *(lds_u32*)(kc + q0 + (g == 3 ? 64 : 2)) = p23;
      unsigned r01 = cvt_pk(d1[0], d1[1]), r23 = cvt_pk(d1[2], d1[3]);
      if (g == 0) { lds_u16* k5 = kc + 512 + rr; k5[0] = (u16)r01; k5[32] = (u16)(r01 >> 16); k5[64] = (u16)r23; k5[96] = (u16)(r23 >> 16); }
      if (lane == 16 && hf == 0) kc[640] = (u16)r01;
      wait_vm<4 + DT>();
      if (lane == 0) lds_st(sm + LDS_SYNC + 16 + hw * 4, 1);
      csync(sm + LDS_SYNC + 40 + 4 * chl, lane);
      auto pk = [](unsigned lo, unsigned hi) { return lo | (hi << 16); };
      auto emit = [&](int x0, u32x4 k0, u32x4 k1) { *(lds_u32x4*)(kb + 512 + 2 * x0) = k0; *(lds_u32x4*)(kb + K1_OFF + 512 + 2 * x0) = k1; };
      auto far8 = [&](int m, unsigned a, unsigned bq) {
        unsigned c = (m & 1) ? bq : a, aa = pk(a, a), bb = pk(bq, bq);
        emit(1024 + 8 * m, u32x4{aa, aa, pk(c, bq), bb}, u32x4{aa, aa, pk(a, c), bb});
      };
      if (lane < 32) *(lds_u32x4*)(kb + hf * K1_OFF + lane * 16) = u32x4{0, 0, 0, 0};
      if (hf == 0) {
        const bool lo = lane < 32;
        const int x0 = 8 * lane, qb = lo ? x0 - 1 : 256 + ((x0 - 256) >> 1);
        unsigned v[9];
        const int pat[9] = {0, 0, 0, 1, 2, 2, 2, 3, 4};
        #pragma unroll
        for (int e = 0; e < 9; ++e) {
          int q = qb + (lo ? e : pat[e]);
          if (e == 0) q = lane == 0 ? 0 : (lane == 32 ? 255 : q);
          v[e] = kc[q];
        }
        if (lane == 0) v[0] = 0;
        emit(x0, u32x4{pk(v[1], v[2]), pk(v[3], v[4]), pk(v[5], v[6]), pk(v[7], v[8])},
                 u32x4{pk(v[0], v[1]), pk(v[2], v[3]), pk(v[4], v[5]), pk(v[6], v[7])});
      } else {
        const int qc = 384 + 2 * lane;
        unsigned a = kc[qc], bq = kc[qc + 1], c = kc[qc + 2];
        unsigned a0 = kc[512 + lane], b0 = kc[513 + lane], a1 = kc[576 + lane], b1 = kc[577 + lane];
        unsigned aa = pk(a, a), bb = pk(bq, bq);
        emit(512 + 8 * lane, u32x4{aa, pk(a, bq), bb, pk(c, c)}, u32x4{aa, aa, bb, pk(bq, c)});
        far8(lane, a0, b0);
        far8(64 + lane, a1, b1);
      }
    }
    wait_vm<0>();
    for (int it = 0; it < SPIN_MAX; ++it) {
      asm volatile("" : "+v"(ur[0]), "+v"(ur[1]), "+v"(ur[2]), "+v"(ur[3]));
      unsigned bad = 0;
      #pragma unroll
      for (int i = 0; i < 4; ++i) bad |= (ur[i][1] ^ tag) | (ur[i][3] ^ tag);
      if (__builtin_amdgcn_ballot_w64(bad != 0) == 0) break;
      uload();
      wait_vm<0>();
    }
    {
      lds_char* ub = kb + UB_OFF + 2048 * hf + 8 * lane - 12;
      #pragma unroll
      for (int i = 0; i < 4; ++i)
        #pragma unroll
        for (int rho = 0; rho < 4; ++rho) {
          lds_u32* p = (lds_u32*)(ub + rho * (U_CP + 4) + 512 * i);
          p[0] = ur[i][0]; p[1] = ur[i][2];
        }
      asm volatile("" ::: "memory");
      if (hf == 0) {
        lds_u32* zt = (lds_u32*)(kb + UB_OFF + g * U_CP);
        zt[1021 + g + j] = 0;
        zt[1037 + g + j < 1039 ? 1037 + g + j : 1039] = 0;
      }
    }
    csync(sm + LDS_SYNC + 32 + 4 * chl, lane);
    __builtin_amdgcn_s_setprio(CVPRIO);
    {
      lds_char* ua = opq(kb + UB_OFF + (j & 3) * U_CP + 2 * (2040 - 8 * (j >> 2) + 8 * g) - 4032);
      lds_char* kp = opq(kb + (j & 1) * K1_OFF + 512 + 16 * g + 64 * (j >> 1) - 448);
      f32x4 Dv[4];
      #pragma unroll
      for (int i = 0; i < 4; ++i) Dv[i] = f32x4{0, 0, 0, 0};
      auto cv = [&](auto HT) {
        constexpr bool H1 = decltype(HT)::value;
        constexpr int n0 = H1 ? 1 : 0, n1 = H1 ? 2 : 3, n2 = H1 ? 5 : 4, n3 = H1 ? 6 : 7;
        bf16x8 A[2][8], B[2][8];
        auto rd = [&](int m0) {
          auto ra = [&](int s) { A[m0 & 1][s] = rd128(ua + 4032 - 64 * m0 - 512 * s); };
          auto rb_ = [&](int q) { B[m0 & 1][q] = rd128(kp + 448 - 64 * m0 + 512 * q); };
          ra(0); rb_(n0); rb_(n1); rb_(n2); rb_(n3);
          ra(1);
          if (H1) { rb_(0); rb_(4); ra(2); rb_(3); } else { rb_(2); rb_(6); ra(2); rb_(1); rb_(5); }
          #pragma unroll
          for (int s = 3; s <= n3; ++s) ra(s);
        };
        rd(0);
        #pragma unroll
        for (int m0 = 0; m0 < 8; ++m0) {
          SCHED0;
          if (m0 + 1 < 8) rd(m0 + 1);
          #pragma unroll
          for (int s = 0; s <= n3; ++s) {
            if (n0 >= s) Dv[0] = mfma16(A[m0 & 1][s], B[m0 & 1][n0 - s], Dv[0]);
            if (n1 >= s) Dv[1] = mfma16(A[m0 & 1][s], B[m0 & 1][n1 - s], Dv[1]);
            if (n2 >= s) Dv[2] = mfma16(A[m0 & 1][s], B[m0 & 1][n2 - s], Dv[2]);
            Dv[3] = mfma16(A[m0 & 1][s], B[m0 & 1][n3 - s], Dv[3]);
          }
          if (m0 + 1 < 8) {
            #pragma unroll
            for (int i = 0; i < 2 * (n3 + 1); ++i) { SGB(0x008, 1); SGB(0x100, 1); }
            SGB(0x008, 4);
          }
          SCHED0;
        }
      };
      if (hf) cv(TrueT{}); else cv(FalseT{});
      const bool odd = j & 1;
      #pragma unroll
      for (int pr = 0; pr < 2; ++pr) {
        const int nb = 4 * pr + (odd ? 3 - hf : hf);
        const int l0 = 256 * nb + 32 * (j >> 1) + 8 * g;
        u32x4 uv = *(lds_u32x4*)(kb + UB_OFF + 3 * U_CP + 2 * (2040 - l0));
        u32x4 o;
        #pragma unroll
        for (int e = 0; e < 4; ++e) {
          float send = odd ? Dv[2 * pr][e] : Dv[2 * pr + 1][e];
          float own = odd ? Dv[2 * pr + 1][e] : Dv[2 * pr][e];
          float recv = __builtin_bit_cast(float, __builtin_amdgcn_update_dpp(0, __builtin_bit_cast(int, send), 0xB1, 0xF, 0xF, true));
          unsigned yp = odd ? cvt_pk(own, recv) : cvt_pk(recv, own);
          unsigned sp = cvt_pk(bfhi(uv[3 - e]) * ds, bflo(uv[3 - e]) * ds);
          o[e] = cvt_pk(bflo(yp) + bflo(sp), bfhi(yp) + bfhi(sp));
        }
        char* dst = ws + WS_A + (size_t)ch * A_PITCH + (8 * nb + (j >> 1)) * 128 + 16 * g;
        store16_sc1(dst, u32x4{o[0], tag, o[1], tag});
        store16_sc1(dst + 64, u32x4{o[2], tag, o[3], tag});
      }
    }
    __builtin_amdgcn_s_setprio(0);
    {
      const char* gq = ws + WS_GATE + (size_t)(64 * rb64 + 16 * hw) * 8;
      const unsigned gof = (2 * g * 2048 + j) * 8;
      u32x2 gt0, gt1;
      for (int it = 0; it < SPIN_MAX; ++it) {
        asm volatile("global_load_dwordx2 %0, %2, %3 sc1\n\tglobal_load_dwordx2 %1, %2, %4 sc1\n\ts_waitcnt vmcnt(0)"
                     : "=&v"(gt0), "=&v"(gt1) : "v"(gof), "s"(gq), "s"(gq + 16384) : "memory");
        if (__builtin_amdgcn_ballot_w64(gt0[1] != tag || gt1[1] != tag) == 0) break;
      }
      const unsigned p0 = gt0[0], p1 = gt1[0];
      const unsigned s2 = __float_as_uint(__uint_as_float(p0) + __uint_as_float(p1));
      const u32x2 r16 = __builtin_amdgcn_permlane16_swap(s2, s2, false, false);
      const unsigned r0 = r16[0], r1 = r16[1];
      const unsigned s4 = __float_as_uint(__uint_as_float(r0) + __uint_as_float(r1));
      const u32x2 r32 = __builtin_amdgcn_permlane32_swap(s4, s4, false, false);
      const unsigned q0 = r32[0], q1 = r32[1];
      const float gs = rbf(__uint_as_float(q0) + __uint_as_float(q1));
      const float gv = rbf(__builtin_amdgcn_rcpf(1.0f + __builtin_amdgcn_exp2f(gs * -1.44269504088896f)));
      if (g == 0) lds_st(sm + LDS_SYNC + 64 + hw * 64 + j * 4, __float_as_uint(gv));
    }
    if (!isG1) static_for<PTS + PTC, PTS + PTC + PTD>([&](auto IT) { ptouch(wtouch, decltype(IT)::value); });
    {
      const char* aq = ws + WS_A + (size_t)(64 * cb + 16 * hw + g) * A_PITCH + rb64 * 256 + j * 16;
      u32x4 av[4];
      for (int it = 0; it < SPIN_MAX; ++it) {
        if (__builtin_amdgcn_readfirstlane(lds_ld(sm + LDS_SYNC + 48 + hw * 4)) != 0) break;
        __builtin_amdgcn_s_sleep(2);
      }
      for (int it = 0; it < SPIN_MAX; ++it) {
        #pragma unroll
        for (int i = 0; i < 4; ++i)
          asm volatile("global_load_dwordx4 %0, %1, off sc1" : "=v"(av[i]) : "v"(aq + (size_t)(4 * i) * A_PITCH) : "memory");
        asm volatile("s_waitcnt vmcnt(0)" : "+v"(av[0]), "+v"(av[1]), "+v"(av[2]), "+v"(av[3]));
        unsigned bad = 0;
        #pragma unroll
        for (int i = 0; i < 4; ++i) bad |= (av[i][1] ^ tag) | (av[i][3] ^ tag);
        if (__builtin_amdgcn_ballot_w64(bad != 0) == 0) break;
        __builtin_amdgcn_s_sleep(ASLEEP);
      }
      asm volatile("" :: "v"(wtouch));
      const int chunk16 = 4 * (j >> 3) + (j & 3), half = (j >> 2) & 1;
      #pragma unroll
      for (int i = 0; i < 4; ++i) {
        const int cl = 4 * i + g;
        *(__attribute__((address_space(3))) u32x2*)(sm + lds_at(hw) + cl * 128 + ((chunk16 ^ (((cl >> 1) & 3) << 1)) << 4) + 8 * half) = u32x2{av[i][0], av[i][2]};
      }
    }
    bar_lgkm();
    if (b == 0 && hw == 0) {
      #pragma unroll
      for (int i = 0; i < 4; ++i) poll_until(ws + WS_A + (size_t)(2 * (64 * i + lane)) * A_PITCH + 4, ~0ull, tag);
      if (lane == 0) store4_sc1(ws, tag);
    }
  }
}

extern "C" void kernel_launch(void* const* d_in, const int* in_sizes, int n_in, void* d_out, int out_size, void* d_ws,
                              size_t ws_size, hipStream_t stream) {
  auto p = [&](int i) { return (const u16*)d_in[i]; };
  fused_kernel<<<256, 512, 0, stream>>>(p(0), p(1), p(2), p(3), p(4), p(5), p(6), p(7), p(8), p(10), p(11), p(12),
                                        (u16*)d_out, (char*)d_ws);
}
```
